```python
import torch

def _softmax(x, axis=-1):
    return torch.softmax(x, dim=axis)
def _gelu(x):
    return torch.nn.functional.gelu(x, approximate="tanh")
def _silu(x):
    return torch.nn.functional.silu(x)
def _sigmoid(x):
    return torch.sigmoid(x)
def _layernorm(x, axis=-1, eps=1e-5):
    m = x.mean(dim=axis, keepdim=True)
    v = x.var(dim=axis, keepdim=True, unbiased=False)
    return (x - m) / torch.sqrt(v + eps)
def _scatter_add(tgt, idx, src, axis=0):
    return tgt.clone().index_add_(axis, idx, src)
def _segment_sum(src, seg, n):
    out = torch.zeros((n,) + src.shape[1:], dtype=src.dtype)
    return out.index_add_(0, seg, src)
def _pad(x, pw):
    flat = tuple(v for pair in reversed(pw) for v in pair)
    return torch.nn.functional.pad(x, flat)

kernel_name = 'interleave_layers__spectral_fnet__glu_gated_glu__prompt'

def setup_inputs():

    _w = (lambda a: (lambda t: t.to(torch.bfloat16) if t.is_floating_point() else t)(torch.as_tensor(a)))
    def _A_setup():
        B, S, D = 1, 2048, 1024
        ns = torch.arange(S)[:, None] * torch.arange(S)[None, :]
        nd = torch.arange(D)[:, None] * torch.arange(D)[None, :]
        Ws_re = torch.cos(-2.0 * torch.pi * ns / S).to(float)
        Ws_im = torch.sin(-2.0 * torch.pi * ns / S).to(float)
        Wd_re = torch.cos(-2.0 * torch.pi * nd / D).to(float)
        Wd_im = torch.sin(-2.0 * torch.pi * nd / D).to(float)
        return {
            "x": _w((lambda *s: torch.randn(*s, dtype=torch.bfloat16) * 0.05)(B, S, D)),
            "dft_seq_re": _w(Ws_re),
            "dft_seq_im": _w(Ws_im),
            "dft_hid_re": _w(Wd_re),
            "dft_hid_im": _w(Wd_im),
        }
    _A_in = _A_setup()

    def _B_setup():
        B, S, D, F = 1, 2048, 1024, 3584
        return {
            "x": _w((lambda *s: torch.randn(*s, dtype=torch.bfloat16) * 0.05)(B, S, D)),
            "w_gate": _w((lambda *s: torch.randn(*s, dtype=torch.bfloat16) * 1.6)(D, F)),
            "w_up": _w((lambda *s: torch.randn(*s, dtype=torch.bfloat16) * 0.8)(D, F)),
            "w_down": _w((lambda *s: torch.randn(*s, dtype=torch.bfloat16) * 0.8)(F, D)),
        }
    _B_in = _B_setup()

    return {
        "A_x": _A_in["x"],
        "A_dft_seq_re": _A_in["dft_seq_re"],
        "A_dft_seq_im": _A_in["dft_seq_im"],
        "A_dft_hid_re": _A_in["dft_hid_re"],
        "A_dft_hid_im": _A_in["dft_hid_im"],
        "B_x": _B_in["x"],
        "B_w_gate": _B_in["w_gate"],
        "B_w_up": _B_in["w_up"],
        "B_w_down": _B_in["w_down"],
    }

def reference(A_x, A_dft_seq_re, A_dft_seq_im, A_dft_hid_re, A_dft_hid_im, B_x, B_w_gate, B_w_up, B_w_down):
    def _A_ref(x, dft_seq_re, dft_seq_im, dft_hid_re, dft_hid_im):
        a = torch.einsum("bsd,ed->bse", x, dft_hid_re)
        b = torch.einsum("bsd,ed->bse", x, dft_hid_im)
        y = torch.einsum("ts,bse->bte", dft_seq_re, a) - torch.einsum("ts,bse->bte", dft_seq_im, b)
        return y

    def _B_ref(x, w_gate, w_up, w_down):
        g = torch.einsum("bsd,df->bsf", x, w_gate)
        u = torch.einsum("bsd,df->bsf", x, w_up)
        h = _sigmoid(g) * u
        return torch.einsum("bsf,fd->bsd", h, w_down)

    x = A_x
    x = x + _A_ref(_layernorm(x, axis=-1), A_dft_seq_re, A_dft_seq_im, A_dft_hid_re, A_dft_hid_im)
    x = x + _B_ref(_layernorm(x, axis=-1), B_w_gate, B_w_up, B_w_down)
    x = x + _A_ref(_layernorm(x, axis=-1), A_dft_seq_re, A_dft_seq_im, A_dft_hid_re, A_dft_hid_im)
    x = x + _B_ref(_layernorm(x, axis=-1), B_w_gate, B_w_up, B_w_down)
    return x
```

```cpp
#include <hip/hip_runtime.h>
#include <hip/hip_bf16.h>
#include <stdint.h>

#define S_LEN 2048
#define D_LEN 1024
#define F_LEN 3584

typedef __attribute__((ext_vector_type(8))) short bf16x8;
typedef __attribute__((ext_vector_type(4))) short s16x4;
typedef __attribute__((ext_vector_type(4))) float f32x4;
typedef __attribute__((address_space(3))) s16x4 lds_s16x4;
typedef unsigned short u16;

__device__ __forceinline__ float bf2f(u16 v) { return __uint_as_float(((unsigned)v) << 16); }
__device__ __forceinline__ u16 f2bf(float f) {
  __hip_bfloat16 h = __float2bfloat16(f);
  return *reinterpret_cast<u16*>(&h);
}
__device__ __forceinline__ float rbf(float f) { return bf2f(f2bf(f)); }

__global__ __launch_bounds__(256) void ln_kernel(const u16* __restrict__ x, u16* __restrict__ xn) {
  const int wave = threadIdx.x >> 6, lane = threadIdx.x & 63;
  const int row = blockIdx.x * 4 + wave;
  const u16* xr = x + (size_t)row * D_LEN;
  float v[16];
  {
    bf16x8 a = *reinterpret_cast<const bf16x8*>(xr + lane * 8);
    bf16x8 b = *reinterpret_cast<const bf16x8*>(xr + 512 + lane * 8);
#pragma unroll
    for (int j = 0; j < 8; ++j) { v[j] = bf2f((u16)a[j]); v[8 + j] = bf2f((u16)b[j]); }
  }
  float s = 0.f;
#pragma unroll
  for (int j = 0; j < 16; ++j) s += v[j];
#pragma unroll
  for (int o = 32; o > 0; o >>= 1) s += __shfl_xor(s, o);
  const float mean = s * (1.0f / D_LEN);
  float q = 0.f;
#pragma unroll
  for (int j = 0; j < 16; ++j) { float d = v[j] - mean; q += d * d; }
#pragma unroll
  for (int o = 32; o > 0; o >>= 1) q += __shfl_xor(q, o);
  const float var = q * (1.0f / D_LEN);
  const float mb = rbf(mean);
  const float vb = rbf(var);
  const float den = rbf(sqrtf(rbf(vb + 1e-5f)));
  bf16x8 o0, o1;
#pragma unroll
  for (int j = 0; j < 8; ++j) {
    o0[j] = (short)f2bf(rbf(v[j] - mb) / den);
    o1[j] = (short)f2bf(rbf(v[8 + j] - mb) / den);
  }
  u16* yr = xn + (size_t)row * D_LEN;
  *reinterpret_cast<bf16x8*>(yr + lane * 8) = o0;
  *reinterpret_cast<bf16x8*>(yr + 512 + lane * 8) = o1;
}

#define BM 128
#define BN 128
#define BK 64
#define STAGE_BYTES 32768
#define NSTAGE 3

#define WAIT_V(n) asm volatile("s_waitcnt vmcnt(" #n ")" ::: "memory")

template <int MODE>
__global__ __launch_bounds__(256) void gemm_kernel(const u16* __restrict__ A0, const u16* __restrict__ A1,
                                                   const u16* __restrict__ B0, const u16* __restrict__ B1,
                                                   u16* __restrict__ out, const u16* __restrict__ resid) {
  extern __shared__ __attribute__((aligned(16))) char smem[];
  constexpr int K = (MODE == 0) ? 1024 : (MODE == 1) ? 4096 : (MODE == 2) ? 1024 : 3584;
  constexpr int NT = K / BK;
  constexpr bool A_TR = (MODE >= 2);
  constexpr int LDA = (MODE == 0) ? 1024 : (MODE == 1) ? 4096 : (MODE == 2) ? F_LEN : D_LEN;
  constexpr int LDB = (MODE == 0) ? 1024 : (MODE == 1) ? 2048 : (MODE == 2) ? 1024 : F_LEN;
  constexpr int MT = (MODE == 0) ? 16 : (MODE == 1) ? 8 : (MODE == 2) ? 56 : 8;

  const int tid = threadIdx.x;
  const int wave = tid >> 6, lane = tid & 63;
  const int wr = wave >> 1, wc = wave & 1;
  const int fr = lane & 15, fq = lane >> 4;
  const int bmi = blockIdx.x % MT, bni = blockIdx.x / MT;
  const int bm0 = bmi * BM, bn0 = bni * BN;

  const int chK = (tid & 7) ^ ((tid >> 4) & 7);
  const u16* bsrc0; const u16* bsrc1;
  {
    const int nrow = bn0 + (tid >> 3);
    if (MODE == 0) {
      const u16* bb = (bn0 < 1024) ? B0 : B1;
      bsrc0 = bb + (size_t)(nrow & 1023) * LDB + chK * 8;
      bsrc1 = bsrc0;
    } else {
      bsrc0 = B0 + (size_t)nrow * LDB + chK * 8;
      bsrc1 = B1 + (size_t)nrow * LDB + chK * 8;
    }
  }
  const u16* asrc;
  if (!A_TR) {
    asrc = A0 + (size_t)(bm0 + (tid >> 3)) * LDA + chK * 8;
  } else {
    const int q = (tid >> 4) & 3, r2 = (tid >> 6) & 3;
    const int ch = (tid & 15) ^ ((q << 2) | r2);
    const int mloc = ch * 8;
    if (MODE == 2) {
      const int w = mloc >> 6, within = mloc & 63;
      const int isUp = within >> 5;
      const int f = bmi * 64 + w * 32 + (within & 31);
      asrc = (isUp ? A1 : A0) + (size_t)(tid >> 4) * LDA + f;
    } else {
      asrc = A0 + (size_t)(tid >> 4) * LDA + bm0 + mloc;
    }
  }

  auto stage = [&](int t, int buf) {
    char* sa = smem + buf * STAGE_BYTES;
    char* sb = sa + 16384;
    const int k0 = t * BK;
#pragma unroll
    for (int i = 0; i < 4; ++i) {
      const u16* g;
      if (!A_TR) g = asrc + (size_t)i * 32 * LDA + k0;
      else g = asrc + (size_t)(k0 + i * 16) * LDA;
      __builtin_amdgcn_global_load_lds((const unsigned*)g, (unsigned*)(sa + (i * 256 + wave * 64) * 16), 16, 0, 0);
    }
#pragma unroll
    for (int i = 0; i < 4; ++i) {
      const u16* g;
      if (MODE == 1) g = (k0 < 2048 ? bsrc0 + k0 : bsrc1 + (k0 - 2048)) + (size_t)i * 32 * LDB;
      else g = bsrc0 + (size_t)i * 32 * LDB + k0;
      __builtin_amdgcn_global_load_lds((const unsigned*)g, (unsigned*)(sb + (i * 256 + wave * 64) * 16), 16, 0, 0);
    }
  };

  f32x4 acc[4][4];
#pragma unroll
  for (int m = 0; m < 4; ++m)
#pragma unroll
    for (int n = 0; n < 4; ++n) acc[m][n] = (f32x4){0.f, 0.f, 0.f, 0.f};

  int aoffK[2], boffK[2];
#pragma unroll
  for (int kk = 0; kk < 2; ++kk) {
    aoffK[kk] = (wr * 64 + fr) * 128 + (((kk * 4 + fq) ^ (fr >> 1)) & 7) * 16;
    boffK[kk] = 16384 + (wc * 64 + fr) * 128 + (((kk * 4 + fq) ^ (fr >> 1)) & 7) * 16;
  }
  int aoffT[4][2];
  if (A_TR) {
    const int g = lane >> 4, q = (lane & 15) >> 2, p = lane & 3;
#pragma unroll
    for (int m = 0; m < 4; ++m)
#pragma unroll
      for (int t = 0; t < 2; ++t) {
        const int c = wr * 4 + m;
        const int X = (q << 2) | ((2 * g + t) & 3);
        aoffT[m][t] = 256 * (8 * g + 4 * t + q) + 16 * ((2 * c + (p >> 1)) ^ X) + 8 * (p & 1);
      }
  }

  stage(0, 0);
  stage(1, 1);

  for (int t = 0; t < NT; ++t) {
    if (t + 1 < NT) { WAIT_V(8); } else { WAIT_V(0); }
    __builtin_amdgcn_s_barrier();
    if (t + 2 < NT) stage(t + 2, (t + 2) % NSTAGE);
    const char* sbase = smem + (t % NSTAGE) * STAGE_BYTES;
#pragma unroll
    for (int kk = 0; kk < 2; ++kk) {
      bf16x8 af[4], bfr[4];
#pragma unroll
      for (int m = 0; m < 4; ++m) {
        if (!A_TR) {
          af[m] = *reinterpret_cast<const bf16x8*>(sbase + aoffK[kk] + m * 16 * 128);
        } else {
          s16x4 lo = __builtin_amdgcn_ds_read_tr16_b64_v4i16((lds_s16x4*)(sbase + aoffT[m][0] + kk * 8192));
          s16x4 hi = __builtin_amdgcn_ds_read_tr16_b64_v4i16((lds_s16x4*)(sbase + aoffT[m][1] + kk * 8192));
          af[m] = (bf16x8){lo[0], lo[1], lo[2], lo[3], hi[0], hi[1], hi[2], hi[3]};
        }
      }
#pragma unroll
      for (int n = 0; n < 4; ++n) bfr[n] = *reinterpret_cast<const bf16x8*>(sbase + boffK[kk] + n * 16 * 128);
#pragma unroll
      for (int m = 0; m < 4; ++m)
#pragma unroll
        for (int n = 0; n < 4; ++n)
          acc[m][n] = __builtin_amdgcn_mfma_f32_16x16x32_bf16(af[m], bfr[n], acc[m][n], 0, 0, 0);
    }
  }

#pragma unroll
  for (int n = 0; n < 4; ++n) {
    const int ng = bn0 + wc * 64 + n * 16 + fr;
    if (MODE == 0) {
      const float sg = (ng >= 1024) ? -1.f : 1.f;
      u16* o = out + (size_t)(ng & 1023) * 4096 + (ng >> 10) * 2048 + bm0 + wr * 64 + fq * 4;
#pragma unroll
      for (int m = 0; m < 4; ++m) {
        s16x4 v;
#pragma unroll
        for (int j = 0; j < 4; ++j) v[j] = (short)f2bf(sg * acc[m][n][j]);
        *reinterpret_cast<s16x4*>(o + m * 16) = v;
      }
    } else if (MODE == 1 || MODE == 3) {
      const size_t base = (size_t)ng * 1024 + bm0 + wr * 64 + fq * 4;
#pragma unroll
      for (int m = 0; m < 4; ++m) {
        s16x4 r = *reinterpret_cast<const s16x4*>(resid + base + m * 16);
        s16x4 v;
#pragma unroll
        for (int j = 0; j < 4; ++j) v[j] = (short)f2bf(bf2f((u16)r[j]) + rbf(acc[m][n][j]));
        *reinterpret_cast<s16x4*>(out + base + m * 16) = v;
      }
    } else {
      u16* o = out + (size_t)ng * F_LEN + bmi * 64 + wr * 32 + fq * 4;
#pragma unroll
      for (int m = 0; m < 2; ++m) {
        s16x4 v;
#pragma unroll
        for (int j = 0; j < 4; ++j) {
          const float g = rbf(acc[m][n][j]);
          const float u = rbf(acc[m + 2][n][j]);
          const float sgm = rbf(1.0f / (1.0f + __expf(-g)));
          v[j] = (short)f2bf(sgm * u);
        }
        *reinterpret_cast<s16x4*>(o + m * 16) = v;
      }
    }
  }
}

extern "C" void kernel_launch(void* const* d_in, const int* in_sizes, int n_in, void* d_out, int out_size,
                              void* d_ws, size_t ws_size, hipStream_t stream) {
  const u16* x0 = (const u16*)d_in[0];
  const u16* seq_re = (const u16*)d_in[1];
  const u16* seq_im = (const u16*)d_in[2];
  const u16* hid_re = (const u16*)d_in[3];
  const u16* hid_im = (const u16*)d_in[4];
  const u16* w_gate = (const u16*)d_in[6];
  const u16* w_up = (const u16*)d_in[7];
  const u16* w_down = (const u16*)d_in[8];
  u16* out = (u16*)d_out;

  char* ws = (char*)d_ws;
  u16* xn = (u16*)(ws);
  u16* abT = (u16*)(ws + (4u << 20));
  u16* xa = (u16*)(ws + (12u << 20));
  u16* xb = (u16*)(ws + (16u << 20));
  u16* h = (u16*)(ws + (20u << 20));

  const size_t shm = STAGE_BYTES * NSTAGE;
  hipFuncSetAttribute((const void*)gemm_kernel<0>, hipFuncAttributeMaxDynamicSharedMemorySize, (int)shm);
  hipFuncSetAttribute((const void*)gemm_kernel<1>, hipFuncAttributeMaxDynamicSharedMemorySize, (int)shm);
  hipFuncSetAttribute((const void*)gemm_kernel<2>, hipFuncAttributeMaxDynamicSharedMemorySize, (int)shm);
  hipFuncSetAttribute((const void*)gemm_kernel<3>, hipFuncAttributeMaxDynamicSharedMemorySize, (int)shm);

  const u16* xcur = x0;
  for (int rep = 0; rep < 2; ++rep) {
    ln_kernel<<<S_LEN / 4, 256, 0, stream>>>(xcur, xn);
    gemm_kernel<0><<<16 * 16, 256, shm, stream>>>(xn, nullptr, hid_re, hid_im, abT, nullptr);
    gemm_kernel<1><<<8 * 16, 256, shm, stream>>>(abT, nullptr, seq_re, seq_im, xa, xcur);
    ln_kernel<<<S_LEN / 4, 256, 0, stream>>>(xa, xn);
    gemm_kernel<2><<<56 * 16, 256, shm, stream>>>(w_gate, w_up, xn, nullptr, h, nullptr);
    u16* dst = (rep == 0) ? xb : out;
    gemm_kernel<3><<<8 * 16, 256, shm, stream>>>(w_down, nullptr, h, nullptr, dst, xa);
    xcur = dst;
  }
}
```

```cpp
#include <hip/hip_runtime.h>
#include <hip/hip_bf16.h>
#include <stdint.h>

#define S_LEN 2048
#define D_LEN 1024
#define F_LEN 3584

typedef __attribute__((ext_vector_type(8))) short bf16x8;
typedef __attribute__((ext_vector_type(4))) short s16x4;
typedef __attribute__((ext_vector_type(4))) float f32x4;
typedef __attribute__((address_space(3))) s16x4 lds_s16x4;
typedef unsigned short u16;

__device__ __forceinline__ float bf2f(u16 v) { return __uint_as_float(((unsigned)v) << 16); }
__device__ __forceinline__ u16 f2bf(float f) {
  __hip_bfloat16 h = __float2bfloat16(f);
  return *reinterpret_cast<u16*>(&h);
}
__device__ __forceinline__ float rbf(float f) { return bf2f(f2bf(f)); }

#define WAIT_V(n) asm volatile("s_waitcnt vmcnt(" #n ")" ::: "memory")
#define WAIT_L0 asm volatile("s_waitcnt lgkmcnt(0)" ::: "memory")
#define BAR __builtin_amdgcn_s_barrier()

template <bool HAS_P, bool DO_LN>
__global__ __launch_bounds__(256) void ln_kernel(const u16* __restrict__ xin, const u16* __restrict__ p0,
                                                 const u16* __restrict__ p1, u16* __restrict__ xout,
                                                 u16* __restrict__ xn) {
  const int wave = threadIdx.x >> 6, lane = threadIdx.x & 63;
  const int row = blockIdx.x * 4 + wave;
  const size_t base = (size_t)row * D_LEN;
  float v[16];
  {
    bf16x8 a = *reinterpret_cast<const bf16x8*>(xin + base + lane * 8);
    bf16x8 b = *reinterpret_cast<const bf16x8*>(xin + base + 512 + lane * 8);
#pragma unroll
    for (int j = 0; j < 8; ++j) { v[j] = bf2f((u16)a[j]); v[8 + j] = bf2f((u16)b[j]); }
  }
  if (HAS_P) {
    bf16x8 a0 = *reinterpret_cast<const bf16x8*>(p0 + base + lane * 8);
    bf16x8 b0 = *reinterpret_cast<const bf16x8*>(p0 + base + 512 + lane * 8);
    bf16x8 a1 = *reinterpret_cast<const bf16x8*>(p1 + base + lane * 8);
    bf16x8 b1 = *reinterpret_cast<const bf16x8*>(p1 + base + 512 + lane * 8);
    bf16x8 o0, o1;
#pragma unroll
    for (int j = 0; j < 8; ++j) {
      v[j] = rbf(v[j] + rbf(bf2f((u16)a0[j]) + bf2f((u16)a1[j])));
      v[8 + j] = rbf(v[8 + j] + rbf(bf2f((u16)b0[j]) + bf2f((u16)b1[j])));
      o0[j] = (short)f2bf(v[j]);
      o1[j] = (short)f2bf(v[8 + j]);
    }
    *reinterpret_cast<bf16x8*>(xout + base + lane * 8) = o0;
    *reinterpret_cast<bf16x8*>(xout + base + 512 + lane * 8) = o1;
  }
  if (!DO_LN) return;
  float s = 0.f;
#pragma unroll
  for (int j = 0; j < 16; ++j) s += v[j];
#pragma unroll
  for (int o = 32; o > 0; o >>= 1) s += __shfl_xor(s, o);
  const float mean = s * (1.0f / D_LEN);
  float q = 0.f;
#pragma unroll
  for (int j = 0; j < 16; ++j) { float d = v[j] - mean; q += d * d; }
#pragma unroll
  for (int o = 32; o > 0; o >>= 1) q += __shfl_xor(q, o);
  const float var = q * (1.0f / D_LEN);
  const float mb = rbf(mean);
  const float vb = rbf(var);
  const float den = rbf(sqrtf(rbf(vb + 1e-5f)));
  bf16x8 o0, o1;
#pragma unroll
  for (int j = 0; j < 8; ++j) {
    o0[j] = (short)f2bf(rbf(v[j] - mb) / den);
    o1[j] = (short)f2bf(rbf(v[8 + j] - mb) / den);
  }
  *reinterpret_cast<bf16x8*>(xn + base + lane * 8) = o0;
  *reinterpret_cast<bf16x8*>(xn + base + 512 + lane * 8) = o1;
}

struct ic0 { static constexpr int value = 0; };
struct ic1 { static constexpr int value = 1; };
#define NST 4
#define STAGE_BYTES 32768

template <int MODE>
__global__ __launch_bounds__(768) void gemm_kernel(const u16* __restrict__ A0, const u16* __restrict__ A1,
                                                   const u16* __restrict__ B0, const u16* __restrict__ B1,
                                                   u16* __restrict__ out) {
  extern __shared__ __attribute__((aligned(16))) char smem[];
  constexpr int KTOT = (MODE == 0) ? 1024 : (MODE == 1) ? 4096 : (MODE == 2) ? 1024 : 3584;
  constexpr int KSPLIT = (MODE == 1 || MODE == 3) ? 2 : 1;
  constexpr int K = KTOT / KSPLIT;
  constexpr int NT = K / 64;
  constexpr bool A_TR = (MODE >= 2);
  constexpr int LDA = (MODE == 0) ? 1024 : (MODE == 1) ? 4096 : (MODE == 2) ? F_LEN : D_LEN;
  constexpr int LDB = (MODE == 0) ? 1024 : (MODE == 1) ? 2048 : (MODE == 2) ? 1024 : F_LEN;
  constexpr int MT = (MODE == 0) ? 16 : (MODE == 1) ? 8 : (MODE == 2) ? 56 : 8;

  const int tid = threadIdx.x, wave = tid >> 6, lane = tid & 63;
  const int grp = __builtin_amdgcn_readfirstlane(wave >> 2);
  const int wr = (wave >> 1) & 1, wc = wave & 1;
  const int fr = lane & 15, fq = lane >> 4;

  int bmi, bni, ks;
  {
    const int xcd = blockIdx.x & 7, idx = blockIdx.x >> 3;
    if (MODE == 0) {
      bmi = (xcd & 3) * 4 + (idx & 3); bni = (xcd >> 2) * 8 + (idx >> 2); ks = 0;
    } else if (MODE == 2) {
      bmi = xcd * 7 + idx % 7; bni = idx / 7; ks = 0;
    } else {
      ks = xcd >> 2; bmi = (xcd & 1) * 4 + (idx & 3); bni = ((xcd >> 1) & 1) * 8 + (idx >> 2);
    }
  }
  const int bm0 = bmi * 128, bn0 = bni * 128;
  const int kbase = ks * K;

  if (grp == 2) {
    const int lt = tid - 512, lw = lt >> 6;
    const int chK = (lt & 7) ^ ((lt >> 4) & 7);
    const u16* bsrc;
    if (MODE == 0) {
      const u16* bb = (bn0 < 1024) ? B0 : B1;
      bsrc = bb + (size_t)((bn0 & 1023) + (lt >> 3)) * LDB + chK * 8;
    } else if (MODE == 1) {
      bsrc = (ks == 0 ? B0 : B1) + (size_t)(bn0 + (lt >> 3)) * LDB + chK * 8;
    } else {
      bsrc = B0 + (size_t)(bn0 + (lt >> 3)) * LDB + kbase + chK * 8;
    }
    const u16* asrc;
    if (!A_TR) {
      asrc = A0 + (size_t)(bm0 + (lt >> 3)) * LDA + kbase + chK * 8;
    } else {
      const int q = (lt >> 4) & 3, r2 = (lt >> 6) & 3;
      const int ch = (lt & 15) ^ ((q << 2) | r2);
      const int mloc = ch * 8;
      if (MODE == 2) {
        const int w = mloc >> 6, within = mloc & 63;
        const int f = bmi * 64 + w * 32 + (within & 31);
        asrc = ((within >> 5) ? A1 : A0) + (size_t)(lt >> 4) * LDA + f;
      } else {
        asrc = A0 + (size_t)(kbase + (lt >> 4)) * LDA + bm0 + mloc;
      }
    }
    auto stA = [&](int t) {
      char* sa = smem + (t % NST) * STAGE_BYTES;
#pragma unroll
      for (int i = 0; i < 4; ++i) {
        const u16* g = A_TR ? asrc + (size_t)(t * 64 + i * 16) * LDA : asrc + (size_t)i * 32 * LDA + t * 64;
        __builtin_amdgcn_global_load_lds((const unsigned*)g, (unsigned*)(sa + (i * 256 + lw * 64) * 16), 16, 0, 0);
      }
    };
    auto stB = [&](int t) {
      char* sa = smem + (t % NST) * STAGE_BYTES + 16384;
#pragma unroll
      for (int i = 0; i < 4; ++i)
        __builtin_amdgcn_global_load_lds((const unsigned*)(bsrc + (size_t)i * 32 * LDB + t * 64),
                                         (unsigned*)(sa + (i * 256 + lw * 64) * 16), 16, 0, 0);
    };
#pragma unroll
    for (int s = 0; s < 3; ++s) { stA(s); stB(s); }
    WAIT_V(16);
    BAR;
    BAR;
    for (int t = 0; t < NT; ++t) {
      if (t + 3 < NT) { stA(t + 3); WAIT_V(12); } else { WAIT_V(0); }
      BAR;
      if (t + 3 < NT) stB(t + 3);
      BAR;
    }
    return;
  }

  f32x4 acc[4][4];
#pragma unroll
  for (int m = 0; m < 4; ++m)
#pragma unroll
    for (int n = 0; n < 4; ++n) acc[m][n] = (f32x4){0.f, 0.f, 0.f, 0.f};
  const int aoff = (wr * 64 + fr) * 128 + (((grp * 4 + fq) ^ (fr >> 1)) & 7) * 16;
  const int boff = 16384 + (wc * 64 + fr) * 128 + (((grp * 4 + fq) ^ (fr >> 1)) & 7) * 16;
  int aoffT[4];
  if (A_TR) {
    const int g = lane >> 4, q = (lane & 15) >> 2, p = lane & 3;
#pragma unroll
    for (int m = 0; m < 4; ++m) {
        const int c = wr * 4 + m;
        const int X = (q << 2) | ((2 * g) & 3);
        aoffT[m] = grp * 8192 + 256 * (8 * g + q) + 16 * ((2 * c + (p >> 1)) ^ X) + 8 * (p & 1);
      }
  }
  bf16x8 af[4], bfr[4];
  auto rdfrag = [&](int t) {
    const char* sb = smem + (t % NST) * STAGE_BYTES;
#pragma unroll
    for (int m = 0; m < 4; ++m) {
      if (!A_TR) {
        af[m] = *reinterpret_cast<const bf16x8*>(sb + aoff + m * 2048);
      } else {
        s16x4 lo = __builtin_amdgcn_ds_read_tr16_b64_v4i16((lds_s16x4*)(sb + aoffT[m]));
        s16x4 hi = __builtin_amdgcn_ds_read_tr16_b64_v4i16((lds_s16x4*)(sb + ((aoffT[m] ^ 16) + 1024)));
        af[m] = (bf16x8){lo[0], lo[1], lo[2], lo[3], hi[0], hi[1], hi[2], hi[3]};
      }
    }
#pragma unroll
    for (int n = 0; n < 4; ++n) bfr[n] = *reinterpret_cast<const bf16x8*>(sb + boff + n * 2048);
  };
  auto mma = [&]() {
    __builtin_amdgcn_s_setprio(1);
#pragma unroll
    for (int m = 0; m < 4; ++m)
#pragma unroll
      for (int n = 0; n < 4; ++n) acc[m][n] = __builtin_amdgcn_mfma_f32_16x16x32_bf16(af[m], bfr[n], acc[m][n], 0, 0, 0);
    __builtin_amdgcn_s_setprio(0);
  };
  float* red = (float*)smem;
  const int tg = tid & 255;
  auto path = [&](auto Gc) {
    constexpr int G = decltype(Gc)::value;
    BAR;
    if (G == 0) { rdfrag(0); WAIT_L0; }
    BAR;
    if (G == 0) {
      for (int t = 0; t < NT; ++t) {
        mma();
        BAR;
        if (t + 1 < NT) { rdfrag(t + 1); }
        WAIT_L0;
        BAR;
      }
    } else {
      for (int t = 0; t < NT; ++t) {
        rdfrag(t);
        WAIT_L0;
        BAR;
        mma();
        BAR;
      }
    }
#pragma unroll
    for (int m = 0; m < 4; ++m)
#pragma unroll
      for (int nn = 0; nn < 2; ++nn)
        *reinterpret_cast<f32x4*>(red + (size_t)((((G * 4 + m) * 2 + nn) * 256) + tg) * 4) = acc[m][(G ^ 1) * 2 + nn];
    __syncthreads();
#pragma unroll
    for (int nn = 0; nn < 2; ++nn) {
#pragma unroll
      for (int m = 0; m < 4; ++m) {
        f32x4 v = *reinterpret_cast<const f32x4*>(red + (size_t)(((((G ^ 1) * 4 + m) * 2 + nn) * 256) + tg) * 4);
        acc[m][G * 2 + nn] += v;
      }
      const int ng = bn0 + wc * 64 + (G * 2 + nn) * 16 + fr;
      if (MODE == 0) {
        const float sg = (ng >= 1024) ? -1.f : 1.f;
        u16* o = out + (size_t)(ng & 1023) * 4096 + (ng >> 10) * 2048 + bm0 + wr * 64 + fq * 4;
#pragma unroll
        for (int m = 0; m < 4; ++m) {
          s16x4 v;
#pragma unroll
          for (int j = 0; j < 4; ++j) v[j] = (short)f2bf(sg * acc[m][G * 2 + nn][j]);
          *reinterpret_cast<s16x4*>(o + m * 16) = v;
        }
      } else if (MODE == 1 || MODE == 3) {
        u16* o = out + (size_t)ks * (S_LEN * D_LEN) + (size_t)ng * 1024 + bm0 + wr * 64 + fq * 4;
#pragma unroll
        for (int m = 0; m < 4; ++m) {
          s16x4 v;
#pragma unroll
          for (int j = 0; j < 4; ++j) v[j] = (short)f2bf(acc[m][G * 2 + nn][j]);
          *reinterpret_cast<s16x4*>(o + m * 16) = v;
        }
      } else {
        u16* o = out + (size_t)ng * F_LEN + bmi * 64 + wr * 32 + fq * 4;
#pragma unroll
        for (int m = 0; m < 2; ++m) {
          s16x4 v;
#pragma unroll
          for (int j = 0; j < 4; ++j) {
            const float g = rbf(acc[m][G * 2 + nn][j]);
            const float u = rbf(acc[m + 2][G * 2 + nn][j]);
            const float sgm = rbf(1.0f / (1.0f + __expf(-g)));
            v[j] = (short)f2bf(sgm * u);
          }
          *reinterpret_cast<s16x4*>(o + m * 16) = v;
        }
      }
    }
  };
  if (grp == 0) path(ic0{}); else path(ic1{});
}

extern "C" void kernel_launch(void* const* d_in, const int* in_sizes, int n_in, void* d_out, int out_size,
                              void* d_ws, size_t ws_size, hipStream_t stream) {
  const u16* x0 = (const u16*)d_in[0];
  const u16* seq_re = (const u16*)d_in[1];
  const u16* seq_im = (const u16*)d_in[2];
  const u16* hid_re = (const u16*)d_in[3];
  const u16* hid_im = (const u16*)d_in[4];
  const u16* w_gate = (const u16*)d_in[6];
  const u16* w_up = (const u16*)d_in[7];
  const u16* w_down = (const u16*)d_in[8];
  u16* out = (u16*)d_out;

  char* ws = (char*)d_ws;
  u16* xn = (u16*)(ws);
  u16* abT = (u16*)(ws + (4u << 20));
  u16* xa = (u16*)(ws + (12u << 20));
  u16* xb = (u16*)(ws + (16u << 20));
  u16* part = (u16*)(ws + (20u << 20));
  u16* h = (u16*)(ws + (28u << 20));

  const size_t shm = STAGE_BYTES * NST;
  hipFuncSetAttribute((const void*)gemm_kernel<0>, hipFuncAttributeMaxDynamicSharedMemorySize, (int)shm);
  hipFuncSetAttribute((const void*)gemm_kernel<1>, hipFuncAttributeMaxDynamicSharedMemorySize, (int)shm);
  hipFuncSetAttribute((const void*)gemm_kernel<2>, hipFuncAttributeMaxDynamicSharedMemorySize, (int)shm);
  hipFuncSetAttribute((const void*)gemm_kernel<3>, hipFuncAttributeMaxDynamicSharedMemorySize, (int)shm);

  u16* p0 = part;
  u16* p1 = part + (size_t)S_LEN * D_LEN;
  ln_kernel<false, true><<<S_LEN / 4, 256, 0, stream>>>(x0, nullptr, nullptr, nullptr, xn);
  gemm_kernel<0><<<256, 768, shm, stream>>>(xn, nullptr, hid_re, hid_im, abT);
  gemm_kernel<1><<<256, 768, shm, stream>>>(abT, nullptr, seq_re, seq_im, part);
  ln_kernel<true, true><<<S_LEN / 4, 256, 0, stream>>>(x0, p0, p1, xa, xn);
  gemm_kernel<2><<<56 * 16, 768, shm, stream>>>(w_gate, w_up, xn, nullptr, h);
  gemm_kernel<3><<<256, 768, shm, stream>>>(w_down, nullptr, h, nullptr, part);
  ln_kernel<true, true><<<S_LEN / 4, 256, 0, stream>>>(xa, p0, p1, xb, xn);
  gemm_kernel<0><<<256, 768, shm, stream>>>(xn, nullptr, hid_re, hid_im, abT);
  gemm_kernel<1><<<256, 768, shm, stream>>>(abT, nullptr, seq_re, seq_im, part);
  ln_kernel<true, true><<<S_LEN / 4, 256, 0, stream>>>(xb, p0, p1, xa, xn);
  gemm_kernel<2><<<56 * 16, 768, shm, stream>>>(w_gate, w_up, xn, nullptr, h);
  gemm_kernel<3><<<256, 768, shm, stream>>>(w_down, nullptr, h, nullptr, part);
  ln_kernel<true, false><<<S_LEN / 4, 256, 0, stream>>>(xa, p0, p1, out, nullptr);
}
```

```cpp
#include <hip/hip_runtime.h>
#include <hip/hip_bf16.h>
#include <stdint.h>

#define S_LEN 2048
#define D_LEN 1024
#define F_LEN 3584

typedef __attribute__((ext_vector_type(8))) short bf16x8;
typedef __attribute__((ext_vector_type(4))) short s16x4;
typedef __attribute__((ext_vector_type(4))) float f32x4;
typedef __attribute__((address_space(3))) s16x4 lds_s16x4;
typedef unsigned short u16;

__device__ __forceinline__ float bf2f(u16 v) { return __uint_as_float(((unsigned)v) << 16); }
__device__ __forceinline__ u16 f2bf(float f) {
  __hip_bfloat16 h = __float2bfloat16(f);
  return *reinterpret_cast<u16*>(&h);
}
__device__ __forceinline__ float rbf(float f) { return bf2f(f2bf(f)); }

#define WAIT_V(n) asm volatile("s_waitcnt vmcnt(" #n ")" ::: "memory")
#define WAIT_L0 asm volatile("s_waitcnt lgkmcnt(0)" ::: "memory")
#define BAR __builtin_amdgcn_s_barrier()

template <bool HAS_P, bool DO_LN>
__global__ __launch_bounds__(256) void ln_kernel(const u16* __restrict__ xin, const u16* __restrict__ p0,
                                                 const u16* __restrict__ p1, u16* __restrict__ xout,
                                                 u16* __restrict__ xn) {
  const int wave = threadIdx.x >> 6, lane = threadIdx.x & 63;
  const int row = blockIdx.x * 4 + wave;
  const size_t base = (size_t)row * D_LEN;
  float v[16];
  {
    bf16x8 a = *reinterpret_cast<const bf16x8*>(xin + base + lane * 8);
    bf16x8 b = *reinterpret_cast<const bf16x8*>(xin + base + 512 + lane * 8);
#pragma unroll
    for (int j = 0; j < 8; ++j) { v[j] = bf2f((u16)a[j]); v[8 + j] = bf2f((u16)b[j]); }
  }
  if (HAS_P) {
    bf16x8 a0 = *reinterpret_cast<const bf16x8*>(p0 + base + lane * 8);
    bf16x8 b0 = *reinterpret_cast<const bf16x8*>(p0 + base + 512 + lane * 8);
    bf16x8 a1 = *reinterpret_cast<const bf16x8*>(p1 + base + lane * 8);
    bf16x8 b1 = *reinterpret_cast<const bf16x8*>(p1 + base + 512 + lane * 8);
    bf16x8 o0, o1;
#pragma unroll
    for (int j = 0; j < 8; ++j) {
      v[j] = rbf(v[j] + rbf(bf2f((u16)a0[j]) + bf2f((u16)a1[j])));
      v[8 + j] = rbf(v[8 + j] + rbf(bf2f((u16)b0[j]) + bf2f((u16)b1[j])));
      o0[j] = (short)f2bf(v[j]);
      o1[j] = (short)f2bf(v[8 + j]);
    }
    *reinterpret_cast<bf16x8*>(xout + base + lane * 8) = o0;
    *reinterpret_cast<bf16x8*>(xout + base + 512 + lane * 8) = o1;
  }
  if (!DO_LN) return;
  float s = 0.f;
#pragma unroll
  for (int j = 0; j < 16; ++j) s += v[j];
#pragma unroll
  for (int o = 32; o > 0; o >>= 1) s += __shfl_xor(s, o);
  const float mean = s * (1.0f / D_LEN);
  float q = 0.f;
#pragma unroll
  for (int j = 0; j < 16; ++j) { float d = v[j] - mean; q += d * d; }
#pragma unroll
  for (int o = 32; o > 0; o >>= 1) q += __shfl_xor(q, o);
  const float var = q * (1.0f / D_LEN);
  const float mb = rbf(mean);
  const float vb = rbf(var);
  const float den = rbf(sqrtf(rbf(vb + 1e-5f)));
  bf16x8 o0, o1;
#pragma unroll
  for (int j = 0; j < 8; ++j) {
    o0[j] = (short)f2bf(rbf(v[j] - mb) / den);
    o1[j] = (short)f2bf(rbf(v[8 + j] - mb) / den);
  }
  *reinterpret_cast<bf16x8*>(xn + base + lane * 8) = o0;
  *reinterpret_cast<bf16x8*>(xn + base + 512 + lane * 8) = o1;
}

struct ic0 { static constexpr int value = 0; };
struct ic1 { static constexpr int value = 1; };
#define NST 4
#define STAGE_BYTES 32768

template <int MODE>
__global__ __launch_bounds__(768) void gemm_kernel(const u16* __restrict__ A0, const u16* __restrict__ A1,
                                                   const u16* __restrict__ B0, const u16* __restrict__ B1,
                                                   u16* __restrict__ out) {
  extern __shared__ __attribute__((aligned(16))) char smem[];
  constexpr int KTOT = (MODE == 0) ? 1024 : (MODE == 1) ? 4096 : (MODE == 2) ? 1024 : 3584;
  constexpr int KSPLIT = (MODE == 1 || MODE == 3) ? 2 : 1;
  constexpr int K = KTOT / KSPLIT;
  constexpr int NT = K / 64;
  constexpr bool A_TR = (MODE >= 2);
  constexpr int LDA = (MODE == 0) ? 1024 : (MODE == 1) ? 4096 : (MODE == 2) ? F_LEN : D_LEN;
  constexpr int LDB = (MODE == 0) ? 1024 : (MODE == 1) ? 2048 : (MODE == 2) ? 1024 : F_LEN;
  constexpr int MT = (MODE == 0) ? 16 : (MODE == 1) ? 8 : (MODE == 2) ? 56 : 8;

  const int tid = threadIdx.x, wave = tid >> 6, lane = tid & 63;
  const int grp = __builtin_amdgcn_readfirstlane(wave >> 2);
  const int wr = (wave >> 1) & 1, wc = wave & 1;
  const int fr = lane & 15, fq = lane >> 4;

  int bmi, bni, ks;
  {
    const int xcd = blockIdx.x & 7, idx = blockIdx.x >> 3;
    if (MODE == 0) {
      bmi = (xcd & 3) * 4 + (idx & 3); bni = (xcd >> 2) * 8 + (idx >> 2); ks = 0;
    } else if (MODE == 2) {
      bmi = xcd * 7 + idx % 7; bni = idx / 7; ks = 0;
    } else {
      ks = xcd >> 2; bmi = (xcd & 1) * 4 + (idx & 3); bni = ((xcd >> 1) & 1) * 8 + (idx >> 2);
    }
  }
  const int bm0 = bmi * 128, bn0 = bni * 128;
  const int kbase = ks * K;

  if (grp == 2) {
    const int lt = tid - 512, lw = lt >> 6;
    const int chK = (lt & 7) ^ ((lt >> 4) & 7);
    const u16* bsrc;
    if (MODE == 0) {
      const u16* bb = (bn0 < 1024) ? B0 : B1;
      bsrc = bb + (size_t)((bn0 & 1023) + (lt >> 3)) * LDB + chK * 8;
    } else if (MODE == 1) {
      bsrc = (ks == 0 ? B0 : B1) + (size_t)(bn0 + (lt >> 3)) * LDB + chK * 8;
    } else {
      bsrc = B0 + (size_t)(bn0 + (lt >> 3)) * LDB + kbase + chK * 8;
    }
    const u16* asrc;
    if (!A_TR) {
      asrc = A0 + (size_t)(bm0 + (lt >> 3)) * LDA + kbase + chK * 8;
    } else {
      const int q = (lt >> 4) & 3, r2 = (lt >> 6) & 3;
      const int ch = (lt & 15) ^ ((q << 2) | r2);
      const int mloc = ch * 8;
      if (MODE == 2) {
        const int w = mloc >> 6, within = mloc & 63;
        const int f = bmi * 64 + w * 32 + (within & 31);
        asrc = ((within >> 5) ? A1 : A0) + (size_t)(lt >> 4) * LDA + f;
      } else {
        asrc = A0 + (size_t)(kbase + (lt >> 4)) * LDA + bm0 + mloc;
      }
    }
    auto stA = [&](int t) {
      char* sa = smem + (t % NST) * STAGE_BYTES;
#pragma unroll
      for (int i = 0; i < 4; ++i) {
        const u16* g = A_TR ? asrc + (size_t)(t * 64 + i * 16) * LDA : asrc + (size_t)i * 32 * LDA + t * 64;
        __builtin_amdgcn_global_load_lds((const unsigned*)g, (unsigned*)(sa + (i * 256 + lw * 64) * 16), 16, 0, 0);
      }
    };
    auto stB = [&](int t) {
      char* sa = smem + (t % NST) * STAGE_BYTES + 16384;
#pragma unroll
      for (int i = 0; i < 4; ++i)
        __builtin_amdgcn_global_load_lds((const unsigned*)(bsrc + (size_t)i * 32 * LDB + t * 64),
                                         (unsigned*)(sa + (i * 256 + lw * 64) * 16), 16, 0, 0);
    };
#pragma unroll
    for (int s = 0; s < 3; ++s) { stA(s); stB(s); }
    WAIT_V(16);
    BAR;
    WAIT_V(8);
    BAR;
    for (int t = 0; t < NT; ++t) {
      if (t + 3 < NT) { stA(t + 3); stB(t + 3); WAIT_V(8); } else { WAIT_V(0); }
      BAR;
    }
    return;
  }

  f32x4 acc[4][4];
#pragma unroll
  for (int m = 0; m < 4; ++m)
#pragma unroll
    for (int n = 0; n < 4; ++n) acc[m][n] = (f32x4){0.f, 0.f, 0.f, 0.f};
  const int aoff = (wr * 64 + fr) * 128;
  const int boff = 16384 + (wc * 64 + fr) * 128;
  const int sw0 = ((fq ^ (fr >> 1)) & 7) * 16, sw1 = (((4 + fq) ^ (fr >> 1)) & 7) * 16;
  int aoffT[4];
  if (A_TR) {
    const int g = lane >> 4, q = (lane & 15) >> 2, p = lane & 3;
#pragma unroll
    for (int m = 0; m < 4; ++m) {
        const int c = wr * 4 + m;
        const int X = (q << 2) | ((2 * g) & 3);
        aoffT[m] = 256 * (8 * g + q) + 16 * ((2 * c + (p >> 1)) ^ X) + 8 * (p & 1);
      }
  }
  bf16x8 af[2][4], bfr[2][4];
  auto rdfrag = [&](int t) {
    const char* sb = smem + (t % NST) * STAGE_BYTES;
#pragma unroll
    for (int kk = 0; kk < 2; ++kk) {
#pragma unroll
      for (int m = 0; m < 4; ++m) {
        if (!A_TR) {
          af[kk][m] = *reinterpret_cast<const bf16x8*>(sb + aoff + m * 2048 + (kk ? sw1 : sw0));
        } else {
          s16x4 lo = __builtin_amdgcn_ds_read_tr16_b64_v4i16((lds_s16x4*)(sb + aoffT[m] + kk * 8192));
          s16x4 hi = __builtin_amdgcn_ds_read_tr16_b64_v4i16((lds_s16x4*)(sb + ((aoffT[m] ^ 16) + 1024) + kk * 8192));
          af[kk][m] = (bf16x8){lo[0], lo[1], lo[2], lo[3], hi[0], hi[1], hi[2], hi[3]};
        }
      }
#pragma unroll
      for (int n = 0; n < 4; ++n) bfr[kk][n] = *reinterpret_cast<const bf16x8*>(sb + boff + n * 2048 + (kk ? sw1 : sw0));
    }
  };
  auto mma = [&]() {
    __builtin_amdgcn_s_setprio(1);
#pragma unroll
    for (int kk = 0; kk < 2; ++kk)
#pragma unroll
      for (int m = 0; m < 4; ++m)
#pragma unroll
        for (int n = 0; n < 4; ++n) acc[m][n] = __builtin_amdgcn_mfma_f32_16x16x32_bf16(af[kk][m], bfr[kk][n], acc[m][n], 0, 0, 0);
    __builtin_amdgcn_s_setprio(0);
  };
  float* red = (float*)smem;
  const int tg = tid & 255;
  auto path = [&](auto Gc) {
    constexpr int G = decltype(Gc)::value;
    BAR;
    if (G == 0) { rdfrag(0); WAIT_L0; }
    BAR;
    if (G == 0) {
      for (int t = 0; t < NT; t += 2) {
        mma();
        BAR;
        if (t + 2 < NT) { rdfrag(t + 2); }
        WAIT_L0;
        BAR;
      }
    } else {
      rdfrag(1);
      WAIT_L0;
      BAR;
      for (int t = 1; t < NT; t += 2) {
        mma();
        BAR;
        if (t + 2 < NT) {
          rdfrag(t + 2);
          WAIT_L0;
          BAR;
        }
      }
    }
#pragma unroll
    for (int m = 0; m < 4; ++m)
#pragma unroll
      for (int nn = 0; nn < 2; ++nn)
        *reinterpret_cast<f32x4*>(red + (size_t)((((G * 4 + m) * 2 + nn) * 256) + tg) * 4) = acc[m][(G ^ 1) * 2 + nn];
    __syncthreads();
#pragma unroll
    for (int nn = 0; nn < 2; ++nn) {
#pragma unroll
      for (int m = 0; m < 4; ++m) {
        f32x4 v = *reinterpret_cast<const f32x4*>(red + (size_t)(((((G ^ 1) * 4 + m) * 2 + nn) * 256) + tg) * 4);
        acc[m][G * 2 + nn] += v;
      }
      const int ng = bn0 + wc * 64 + (G * 2 + nn) * 16 + fr;
      if (MODE == 0) {
        const float sg = (ng >= 1024) ? -1.f : 1.f;
        u16* o = out + (size_t)(ng & 1023) * 4096 + (ng >> 10) * 2048 + bm0 + wr * 64 + fq * 4;
#pragma unroll
        for (int m = 0; m < 4; ++m) {
          s16x4 v;
#pragma unroll
          for (int j = 0; j < 4; ++j) v[j] = (short)f2bf(sg * acc[m][G * 2 + nn][j]);
          *reinterpret_cast<s16x4*>(o + m * 16) = v;
        }
      } else if (MODE == 1 || MODE == 3) {
        u16* o = out + (size_t)ks * (S_LEN * D_LEN) + (size_t)ng * 1024 + bm0 + wr * 64 + fq * 4;
#pragma unroll
        for (int m = 0; m < 4; ++m) {
          s16x4 v;
#pragma unroll
          for (int j = 0; j < 4; ++j) v[j] = (short)f2bf(acc[m][G * 2 + nn][j]);
          *reinterpret_cast<s16x4*>(o + m * 16) = v;
        }
      } else {
        u16* o = out + (size_t)ng * F_LEN + bmi * 64 + wr * 32 + fq * 4;
#pragma unroll
        for (int m = 0; m < 2; ++m) {
          s16x4 v;
#pragma unroll
          for (int j = 0; j < 4; ++j) {
            const float g = rbf(acc[m][G * 2 + nn][j]);
            const float u = rbf(acc[m + 2][G * 2 + nn][j]);
            const float sgm = rbf(1.0f / (1.0f + __expf(-g)));
            v[j] = (short)f2bf(sgm * u);
          }
          *reinterpret_cast<s16x4*>(o + m * 16) = v;
        }
      }
    }
  };
  if (grp == 0) path(ic0{}); else path(ic1{});
}


#define G3_STAGE 49152
__global__ __launch_bounds__(768) void g3_kernel(const u16* __restrict__ Wg, const u16* __restrict__ Wu,
                                                 const u16* __restrict__ X, u16* __restrict__ H) {
  extern __shared__ __attribute__((aligned(16))) char smem[];
  constexpr int NT = D_LEN / 64;
  const int tid = threadIdx.x, wave = tid >> 6, lane = tid & 63;
  const int grp = __builtin_amdgcn_readfirstlane(wave >> 2);
  const int wr = (wave >> 1) & 3, wc = wave & 1;
  const int fr = lane & 15, fq = lane >> 4;
  const int xcd = blockIdx.x & 7, idx = blockIdx.x >> 3;
  const int bfi = (xcd & 3) * 7 + idx % 7, bsi = (xcd >> 2) * 8 + idx / 7;
  const int f0 = bfi * 128, s0 = bsi * 128;
  if (grp == 2) {
    const int lt = tid - 512, lw = lt >> 6;
    const u16* bsrc = X + (size_t)(s0 + (lt >> 3)) * D_LEN + ((lt & 7) ^ ((lt >> 4) & 7)) * 8;
    const int q = (lt >> 4) & 3, r2 = (lt >> 6) & 3;
    const int ch = (lt & 15) ^ ((q << 2) | r2);
    const int mloc = ch * 8, w = mloc >> 6, within = mloc & 63;
    const u16* asrc = ((within >> 5) ? Wu : Wg) + (size_t)(lt >> 4) * F_LEN + f0 + w * 32 + (within & 31);
    auto stA = [&](int t) {
      char* sa = smem + (t % 3) * G3_STAGE;
#pragma unroll
      for (int hh = 0; hh < 2; ++hh)
#pragma unroll
        for (int i = 0; i < 4; ++i)
          __builtin_amdgcn_global_load_lds((const unsigned*)(asrc + (size_t)(t * 64 + i * 16) * F_LEN + hh * 64),
                                           (unsigned*)(sa + hh * 16384 + (i * 256 + lw * 64) * 16), 16, 0, 0);
    };
    auto stB = [&](int t) {
      char* sa = smem + (t % 3) * G3_STAGE + 32768;
#pragma unroll
      for (int i = 0; i < 4; ++i)
        __builtin_amdgcn_global_load_lds((const unsigned*)(bsrc + (size_t)i * 32 * D_LEN + t * 64),
                                         (unsigned*)(sa + (i * 256 + lw * 64) * 16), 16, 0, 0);
    };
    stA(0); stB(0); stA(1); stB(1);
    WAIT_V(12);
    BAR;
    BAR;
    for (int t = 0; t < NT; ++t) {
      if (t + 2 < NT) { stA(t + 2); WAIT_V(8); } else { WAIT_V(0); }
      BAR;
      if (t + 2 < NT) stB(t + 2);
      BAR;
    }
    return;
  }
  f32x4 acc[4][4];
#pragma unroll
  for (int m = 0; m < 4; ++m)
#pragma unroll
    for (int n = 0; n < 4; ++n) acc[m][n] = (f32x4){0.f, 0.f, 0.f, 0.f};
  const int boff = 32768 + (wc * 64 + fr) * 128;
  const int sw0 = ((fq ^ (fr >> 1)) & 7) * 16, sw1 = (((4 + fq) ^ (fr >> 1)) & 7) * 16;
  int aoffT[4];
  {
    const int g = lane >> 4, q = (lane & 15) >> 2, p = lane & 3;
#pragma unroll
    for (int m = 0; m < 4; ++m) {
      const int c = (wr & 1) * 4 + m;
      const int X_ = (q << 2) | ((2 * g) & 3);
      aoffT[m] = (wr >> 1) * 16384 + 256 * (8 * g + q) + 16 * ((2 * c + (p >> 1)) ^ X_) + 8 * (p & 1);
    }
  }
  bf16x8 af[2][4], bfr[2][4];
  auto rdfrag = [&](int t) {
    const char* sb = smem + (t % 3) * G3_STAGE;
#pragma unroll
    for (int kk = 0; kk < 2; ++kk) {
#pragma unroll
      for (int m = 0; m < 4; ++m) {
        s16x4 lo = __builtin_amdgcn_ds_read_tr16_b64_v4i16((lds_s16x4*)(sb + aoffT[m] + kk * 8192));
        s16x4 hi = __builtin_amdgcn_ds_read_tr16_b64_v4i16((lds_s16x4*)(sb + ((aoffT[m] ^ 16) + 1024) + kk * 8192));
        af[kk][m] = (bf16x8){lo[0], lo[1], lo[2], lo[3], hi[0], hi[1], hi[2], hi[3]};
      }
#pragma unroll
      for (int n = 0; n < 4; ++n) bfr[kk][n] = *reinterpret_cast<const bf16x8*>(sb + boff + n * 2048 + (kk ? sw1 : sw0));
    }
  };
  auto mma = [&]() {
    __builtin_amdgcn_s_setprio(1);
#pragma unroll
    for (int kk = 0; kk < 2; ++kk)
#pragma unroll
      for (int m = 0; m < 4; ++m)
#pragma unroll
        for (int n = 0; n < 4; ++n) acc[m][n] = __builtin_amdgcn_mfma_f32_16x16x32_bf16(af[kk][m], bfr[kk][n], acc[m][n], 0, 0, 0);
    __builtin_amdgcn_s_setprio(0);
  };
  auto path = [&](auto Gc) {
    constexpr int G = decltype(Gc)::value;
    BAR;
    if (G == 0) { rdfrag(0); WAIT_L0; }
    BAR;
    if (G == 0) {
      for (int t = 0; t < NT; ++t) {
        mma();
        BAR;
        if (t + 1 < NT) { rdfrag(t + 1); }
        WAIT_L0;
        BAR;
      }
    } else {
      for (int t = 0; t < NT; ++t) {
        rdfrag(t);
        WAIT_L0;
        BAR;
        mma();
        BAR;
      }
    }
#pragma unroll
    for (int n = 0; n < 4; ++n) {
      u16* o = H + (size_t)(s0 + wc * 64 + n * 16 + fr) * F_LEN + f0 + wr * 32 + fq * 4;
#pragma unroll
      for (int m = 0; m < 2; ++m) {
        s16x4 v;
#pragma unroll
        for (int j = 0; j < 4; ++j) {
          const float g = rbf(acc[m][n][j]);
          const float u = rbf(acc[m + 2][n][j]);
          const float sgm = rbf(1.0f / (1.0f + __expf(-g)));
          v[j] = (short)f2bf(sgm * u);
        }
        *reinterpret_cast<s16x4*>(o + m * 16) = v;
      }
    }
  };
  if (grp == 0) path(ic0{}); else path(ic1{});
}

extern "C" void kernel_launch(void* const* d_in, const int* in_sizes, int n_in, void* d_out, int out_size,
                              void* d_ws, size_t ws_size, hipStream_t stream) {
  const u16* x0 = (const u16*)d_in[0];
  const u16* seq_re = (const u16*)d_in[1];
  const u16* seq_im = (const u16*)d_in[2];
  const u16* hid_re = (const u16*)d_in[3];
  const u16* hid_im = (const u16*)d_in[4];
  const u16* w_gate = (const u16*)d_in[6];
  const u16* w_up = (const u16*)d_in[7];
  const u16* w_down = (const u16*)d_in[8];
  u16* out = (u16*)d_out;

  char* ws = (char*)d_ws;
  u16* xn = (u16*)(ws);
  u16* abT = (u16*)(ws + (4u << 20));
  u16* xa = (u16*)(ws + (12u << 20));
  u16* xb = (u16*)(ws + (16u << 20));
  u16* part = (u16*)(ws + (20u << 20));
  u16* h = (u16*)(ws + (28u << 20));

  const size_t shm = STAGE_BYTES * NST;
  hipFuncSetAttribute((const void*)gemm_kernel<0>, hipFuncAttributeMaxDynamicSharedMemorySize, (int)shm);
  hipFuncSetAttribute((const void*)gemm_kernel<1>, hipFuncAttributeMaxDynamicSharedMemorySize, (int)shm);
  hipFuncSetAttribute((const void*)g3_kernel, hipFuncAttributeMaxDynamicSharedMemorySize, 3 * G3_STAGE);
  hipFuncSetAttribute((const void*)gemm_kernel<3>, hipFuncAttributeMaxDynamicSharedMemorySize, (int)shm);

  u16* p0 = part;
  u16* p1 = part + (size_t)S_LEN * D_LEN;
  ln_kernel<false, true><<<S_LEN / 4, 256, 0, stream>>>(x0, nullptr, nullptr, nullptr, xn);
  gemm_kernel<0><<<256, 768, shm, stream>>>(xn, nullptr, hid_re, hid_im, abT);
  gemm_kernel<1><<<256, 768, shm, stream>>>(abT, nullptr, seq_re, seq_im, part);
  ln_kernel<true, true><<<S_LEN / 4, 256, 0, stream>>>(x0, p0, p1, xa, xn);
  g3_kernel<<<448, 768, 3 * G3_STAGE, stream>>>(w_gate, w_up, xn, h);
  gemm_kernel<3><<<256, 768, shm, stream>>>(w_down, nullptr, h, nullptr, part);
  ln_kernel<true, true><<<S_LEN / 4, 256, 0, stream>>>(xa, p0, p1, xb, xn);
  gemm_kernel<0><<<256, 768, shm, stream>>>(xn, nullptr, hid_re, hid_im, abT);
  gemm_kernel<1><<<256, 768, shm, stream>>>(abT, nullptr, seq_re, seq_im, part);
  ln_kernel<true, true><<<S_LEN / 4, 256, 0, stream>>>(xb, p0, p1, xa, xn);
  g3_kernel<<<448, 768, 3 * G3_STAGE, stream>>>(w_gate, w_up, xn, h);
  gemm_kernel<3><<<256, 768, shm, stream>>>(w_down, nullptr, h, nullptr, part);
  ln_kernel<true, false><<<S_LEN / 4, 256, 0, stream>>>(xa, p0, p1, out, nullptr);
}
```
